```python
import torch

def _softmax(x, axis=-1):
    return torch.softmax(x, dim=axis)
def _gelu(x):
    return torch.nn.functional.gelu(x, approximate="tanh")
def _silu(x):
    return torch.nn.functional.silu(x)
def _sigmoid(x):
    return torch.sigmoid(x)
def _layernorm(x, axis=-1, eps=1e-5):
    m = x.mean(dim=axis, keepdim=True)
    v = x.var(dim=axis, keepdim=True, unbiased=False)
    return (x - m) / torch.sqrt(v + eps)
def _scatter_add(tgt, idx, src, axis=0):
    return tgt.clone().index_add_(axis, idx, src)
def _segment_sum(src, seg, n):
    out = torch.zeros((n,) + src.shape[1:], dtype=src.dtype)
    return out.index_add_(0, seg, src)
def _pad(x, pw):
    flat = tuple(v for pair in reversed(pw) for v in pair)
    return torch.nn.functional.pad(x, flat)

kernel_name = 'concat_heads__mha_block_cached__mha_block_cached__near_ridge'

def setup_inputs():

    _w = (lambda a: (lambda t: t.to(torch.bfloat16) if t.is_floating_point() else t)(torch.as_tensor(a)))
    def _A_setup():
        B, Sq, D = 8, 144, 448
        H, Sk, Dh = 16, 512, 256
        return {
            "x": _w((lambda *s: torch.randn(*s, dtype=torch.bfloat16) * 0.8)(B, Sq, D)),
            "k_cache": _w((lambda *s: torch.randn(*s, dtype=torch.bfloat16) * 0.8)(B, H, Sk, Dh)),
            "v_cache": _w((lambda *s: torch.randn(*s, dtype=torch.bfloat16) * 0.05)(B, H, Sk, Dh)),
            "wq": _w((lambda *s: torch.randn(*s, dtype=torch.bfloat16) * 0.8)(D, H * Dh) * (D ** -0.5)),
            "wo": _w((lambda *s: torch.randn(*s, dtype=torch.bfloat16) * 0.05)(H * Dh, D) * ((H * Dh) ** -0.5)),
        }
    _A_in = _A_setup()

    def _B_setup():
        B, Sq, D = 8, 144, 448
        H, Sk, Dh = 16, 512, 256
        return {
            "x": _w((lambda *s: torch.randn(*s, dtype=torch.bfloat16) * 0.8)(B, Sq, D)),
            "k_cache": _w((lambda *s: torch.randn(*s, dtype=torch.bfloat16) * 0.8)(B, H, Sk, Dh)),
            "v_cache": _w((lambda *s: torch.randn(*s, dtype=torch.bfloat16) * 0.05)(B, H, Sk, Dh)),
            "wq": _w((lambda *s: torch.randn(*s, dtype=torch.bfloat16) * 0.8)(D, H * Dh) * (D ** -0.5)),
            "wo": _w((lambda *s: torch.randn(*s, dtype=torch.bfloat16) * 0.05)(H * Dh, D) * ((H * Dh) ** -0.5)),
        }
    _B_in = _B_setup()

    return {
        "A_x": _A_in["x"],
        "A_k_cache": _A_in["k_cache"],
        "A_v_cache": _A_in["v_cache"],
        "A_wq": _A_in["wq"],
        "A_wo": _A_in["wo"],
        "B_x": _B_in["x"],
        "B_k_cache": _B_in["k_cache"],
        "B_v_cache": _B_in["v_cache"],
        "B_wq": _B_in["wq"],
        "B_wo": _B_in["wo"],
    }

def reference(A_x, A_k_cache, A_v_cache, A_wq, A_wo, B_x, B_k_cache, B_v_cache, B_wq, B_wo):
    def _A_ref(x, k_cache, v_cache, wq, wo):
        B, Sq, D = x.shape
        H, Dh = 16, 256
        q = (lambda x, *d: x.permute(*d))((x @ wq).reshape(B, Sq, H, Dh), 0, 2, 1, 3)
        scores = torch.einsum("bhqd,bhkd->bhqk", q, k_cache) * (Dh ** -0.5)
        p = _softmax(scores, axis=-1)
        o = torch.einsum("bhqk,bhkd->bhqd", p, v_cache)
        return (lambda x, *d: x.permute(*d))(o, 0, 2, 1, 3).reshape(B, Sq, H * Dh) @ wo

    def _B_ref(x, k_cache, v_cache, wq, wo):
        B, Sq, D = x.shape
        H, Dh = 16, 256
        q = (lambda x, *d: x.permute(*d))((x @ wq).reshape(B, Sq, H, Dh), 0, 2, 1, 3)
        scores = torch.einsum("bhqd,bhkd->bhqk", q, k_cache) * (Dh ** -0.5)
        p = _softmax(scores, axis=-1)
        o = torch.einsum("bhqk,bhkd->bhqd", p, v_cache)
        return (lambda x, *d: x.permute(*d))(o, 0, 2, 1, 3).reshape(B, Sq, H * Dh) @ wo

    a = _A_ref(A_x, A_k_cache, A_v_cache, A_wq, A_wo)
    b = _B_ref(B_x, B_k_cache, B_v_cache, B_wq, B_wo)
    return torch.concatenate([a, b], axis=-1)
```

```cpp
#include <hip/hip_runtime.h>
#include <hip/hip_bf16.h>
#include <stdint.h>

typedef __attribute__((ext_vector_type(8))) short bf16x8;
typedef __attribute__((ext_vector_type(4))) short bf16x4;
typedef __attribute__((ext_vector_type(4))) float f32x4;
typedef __attribute__((ext_vector_type(2))) unsigned u32x2;
typedef __attribute__((ext_vector_type(4))) unsigned u32x4;
typedef unsigned short u16;

namespace {
constexpr int NB = 8, SQ = 144, DM = 448, NH = 16, SK = 512, DH = 256, HD = NH * DH;
constexpr int LDS_BYTES = 163840;
constexpr int SLOT = 32768;
constexpr int SLAB_OFF = 98304;
constexpr int SCR_OFF = 131072;
constexpr float SM_C = 0.0625f * 1.4426950408889634f;

#define MFMA32(a, b, c) __builtin_amdgcn_mfma_f32_16x16x32_bf16((a), (b), (c), 0, 0, 0)
#define MFMA16(a, b, c) __builtin_amdgcn_mfma_f32_16x16x16bf16_1k((a), (b), (c), 0, 0, 0)

__device__ __forceinline__ void dma16(const void* gbase  , unsigned voff, unsigned lds_dst) {
  unsigned keep;
  asm volatile("s_mov_b32 %0, m0\n\ts_mov_b32 m0, %3\n\ts_nop 0\n\tglobal_load_lds_dwordx4 %1, %2\n\ts_mov_b32 m0, %0"
               : "=&s"(keep) : "v"(voff), "s"(gbase), "s"(lds_dst) : "memory");
}
template <int N> __device__ __forceinline__ void wait_bar() {
  asm volatile("s_waitcnt vmcnt(%0) lgkmcnt(0)" :: "n"(N) : "memory");
  __builtin_amdgcn_s_barrier();
  asm volatile("" ::: "memory");
}
__device__ __forceinline__ unsigned cvtpk(float lo, float hi) {
  unsigned r; asm("v_cvt_pk_bf16_f32 %0, %1, %2" : "=v"(r) : "v"(lo), "v"(hi)); return r;
}
__device__ __forceinline__ bf16x4 tr_rd(const char* smem, unsigned off) {
  return __builtin_amdgcn_ds_read_tr16_b64_v4i16((__attribute__((address_space(3))) bf16x4*)(smem + off));
}
__device__ __forceinline__ bf16x8 cat8(bf16x4 a, bf16x4 b) {
  return (bf16x8){a[0], a[1], a[2], a[3], b[0], b[1], b[2], b[3]};
}
__device__ __forceinline__ float xmax(float v) {
  v = fmaxf(v, __shfl_xor(v, 16)); v = fmaxf(v, __shfl_xor(v, 32)); return v;
}
__device__ __forceinline__ float xsum(float v) {
  v += __shfl_xor(v, 16); v += __shfl_xor(v, 32); return v;
}

constexpr int kq_cnt(int s) { return (s >= 0 && s <= 11) ? 2 : 0; }
constexpr int slab_cnt(int s) { return (s + 3 <= 13) ? 4 : 0; }
constexpr int younger_than_slab(int s) {
  int n = 0;
  if (s < 3) { n += (2 - s) * 4; for (int t = 0; t < s; ++t) n += slab_cnt(t) + kq_cnt(t); }
  else { n += kq_cnt(s - 3); for (int t = s - 2; t < s; ++t) n += slab_cnt(t) + kq_cnt(t); }
  return n;
}
}

struct Ctx {
  char* smem;
  unsigned smem_base;
  int w, lane, c, g;
  const char* kv[2];
};

template <int ISV>
__device__ __forceinline__ void issue_kv(const Ctx& X, int chunk, int slot, int n0, int n1) {
  const char* base = X.kv[ISV] + (size_t)chunk * SLOT;
  int ln = X.lane; asm volatile("" : "+v"(ln));
#pragma unroll
  for (int n = n0; n < n1; ++n) {
    const int row = 16 * X.w + 2 * n + (ln >> 5);
    const int p = ln & 31;
    const int sc = ISV ? (p ^ ((row & 7) << 1)) : (p ^ (row & 15));
    dma16(base, row * 512 + sc * 16, X.smem_base + slot * SLOT + X.w * 8192 + n * 1024);
  }
}

__global__ __launch_bounds__(256, 1)
void attn_fused(const u16* __restrict__ xA, const u16* __restrict__ kA, const u16* __restrict__ vA, const u16* __restrict__ wqA,
                const u16* __restrict__ xB, const u16* __restrict__ kB, const u16* __restrict__ vB, const u16* __restrict__ wqB,
                u16* __restrict__ o_ws) {
  __shared__ __attribute__((aligned(16))) char smem[LDS_BYTES];
  const int bid = blockIdx.x;
  const int h = bid & 15, b = (bid >> 4) & 7, blk = bid >> 7;
  const u16* x = (blk ? xB : xA) + (size_t)b * SQ * DM;
  const u16* wq = (blk ? wqB : wqA) + h * DH;
  Ctx X;
  X.smem = smem;
  X.smem_base = (unsigned)(uintptr_t)smem;
  X.w = __builtin_amdgcn_readfirstlane(threadIdx.x >> 6);
  X.lane = threadIdx.x & 63; X.c = X.lane & 15; X.g = X.lane >> 4;
  X.kv[0] = (const char*)((blk ? kB : kA) + (size_t)(b * NH + h) * SK * DH);
  X.kv[1] = (const char*)((blk ? vB : vA) + (size_t)(b * NH + h) * SK * DH);
  const int w = X.w, lane = X.lane, c = X.c, g = X.g;

  bf16x8 xf[3][14];
  {
    const u16* r0 = x + (size_t)(32 * w + c) * DM + 8 * g;
    const u16* r1 = r0 + 16 * DM;
    const u16* r9 = x + (size_t)(128 + c) * DM + 8 * g;
#pragma unroll
    for (int s = 0; s < 14; ++s) {
      xf[0][s] = *(const bf16x8*)(r0 + 32 * s);
      xf[1][s] = *(const bf16x8*)(r1 + 32 * s);
      xf[2][s] = *(const bf16x8*)(r9 + 32 * s);
    }
  }
  auto issue_slab = [&](int s) {
    int ln = lane; asm volatile("" : "+v"(ln));
#pragma unroll
    for (int n = 0; n < 4; ++n) {
      const int row = 8 * w + 2 * n + (ln >> 5);
      const int p = ln & 31;
      const int f = (row & 3) | (((row >> 3) & 1) << 2);
      const int sc = (((p >> 1) ^ f) << 1) | (p & 1);
      dma16((const char*)(wq + (size_t)(32 * s) * HD), row * (HD * 2) + sc * 16,
            X.smem_base + SLAB_OFF + (s & 3) * 16384 + w * 4096 + n * 1024);
    }
  };
  issue_slab(0); issue_slab(1); issue_slab(2);

  f32x4 qa0[16], qa1[16], qa9[4];
#pragma unroll
  for (int t = 0; t < 16; ++t) { qa0[t] = (f32x4){0, 0, 0, 0}; qa1[t] = (f32x4){0, 0, 0, 0}; }
#pragma unroll
  for (int t = 0; t < 4; ++t) qa9[t] = (f32x4){0, 0, 0, 0};

  const int qq = c >> 2, pp = c & 3;
#pragma unroll
  for (int s = 0; s < 14; ++s) {
    switch (s) {
#define WB(S) case S: wait_bar<younger_than_slab(S)>(); break;
      WB(0) WB(1) WB(2) WB(3) WB(4) WB(5) WB(6) WB(7) WB(8) WB(9) WB(10) WB(11) WB(12) WB(13)
#undef WB
    }
    if (s + 3 <= 13) issue_slab(s + 3);
    if (s <= 11) issue_kv<0>(X, s >> 2, s >> 2, 2 * (s & 3), 2 * (s & 3) + 2);
    const unsigned sb = SLAB_OFF + (s & 3) * 16384;
#pragma unroll
    for (int t = 0; t < 16; ++t) {
      const int ta = (t + 4 * w) & 15;
      bf16x4 a0, a1;
      {
        const int row = 8 * g + qq;
        const int f = (row & 3) | (((row >> 3) & 1) << 2);
        a0 = tr_rd(smem, sb + row * 512 + ((ta ^ f) * 32) + pp * 8);
        const int row1 = row + 4;
        const int f1 = (row1 & 3) | (((row1 >> 3) & 1) << 2);
        a1 = tr_rd(smem, sb + row1 * 512 + ((ta ^ f1) * 32) + pp * 8);
      }
      const bf16x8 a = cat8(a0, a1);
      qa0[t] = MFMA32(a, xf[0][s], qa0[t]);
      qa1[t] = MFMA32(a, xf[1][s], qa1[t]);
      if (t < 4) qa9[t] = MFMA32(a, xf[2][s], qa9[t]);
    }
  }

  bf16x8 q0[8], q1[8], q9[8];
  {
    char* pr = smem + SCR_OFF + w * 8192;
#pragma unroll
    for (int t = 0; t < 16; ++t) {
      const int ta = (t + 4 * w) & 15;
      const int ch = 2 * ta + (g >> 1);
      u32x2 v = {cvtpk(qa0[t][0], qa0[t][1]), cvtpk(qa0[t][2], qa0[t][3])};
      *(u32x2*)(pr + c * 512 + ((ch ^ c) * 16) + (g & 1) * 8) = v;
    }
#pragma unroll
    for (int ks = 0; ks < 8; ++ks) q0[ks] = *(const bf16x8*)(pr + c * 512 + (((4 * ks + g) ^ c) * 16));
    asm volatile("s_waitcnt lgkmcnt(0)" ::: "memory");
#pragma unroll
    for (int t = 0; t < 16; ++t) {
      const int ta = (t + 4 * w) & 15;
      const int ch = 2 * ta + (g >> 1);
      u32x2 v = {cvtpk(qa1[t][0], qa1[t][1]), cvtpk(qa1[t][2], qa1[t][3])};
      *(u32x2*)(pr + c * 512 + ((ch ^ c) * 16) + (g & 1) * 8) = v;
    }
#pragma unroll
    for (int ks = 0; ks < 8; ++ks) q1[ks] = *(const bf16x8*)(pr + c * 512 + (((4 * ks + g) ^ c) * 16));
    wait_bar<63>();
    char* sh = smem + SLAB_OFF;
#pragma unroll
    for (int t = 0; t < 4; ++t) {
      const int ta = t + 4 * w;
      const int ch = 2 * ta + (g >> 1);
      u32x2 v = {cvtpk(qa9[t][0], qa9[t][1]), cvtpk(qa9[t][2], qa9[t][3])};
      *(u32x2*)(sh + c * 512 + ((ch ^ c) * 16) + (g & 1) * 8) = v;
    }
    wait_bar<63>();
#pragma unroll
    for (int ks = 0; ks < 8; ++ks) q9[ks] = *(const bf16x8*)(sh + c * 512 + (((4 * ks + g) ^ c) * 16));
  }

  f32x4 S0[8][4], S1[8][4], S9[8];
#pragma unroll
  for (int i = 0; i < 8; ++i) {
    wait_bar<16>();
    if (i + 3 < 8) issue_kv<0>(X, i + 3, (i + 3) & 3, 0, 8);
    else issue_kv<1>(X, i + 3 - 8, (i + 3) & 3, 0, 8);
    const unsigned sb = (i & 3) * SLOT;
#pragma unroll
    for (int kt = 0; kt < 4; ++kt) {
      const int kta = (kt + w) & 3;
      f32x4 s0 = {0, 0, 0, 0}, s1 = {0, 0, 0, 0}, s9 = {0, 0, 0, 0};
#pragma unroll
      for (int ks = 0; ks < 8; ++ks) {
        const bf16x8 a = *(const bf16x8*)(smem + sb + (16 * kta + c) * 512 + (((4 * ks + g) ^ c) * 16));
        s0 = MFMA32(a, q0[ks], s0);
        s1 = MFMA32(a, q1[ks], s1);
        if (kt == 0) s9 = MFMA32(a, q9[ks], s9);
      }
      S0[i][kt] = s0; S1[i][kt] = s1;
      if (kt == 0) S9[i] = s9;
    }
  }

  float m0 = S0[0][0][0], m1 = S1[0][0][0], m9 = S9[0][0];
#pragma unroll
  for (int i = 0; i < 8; ++i) {
#pragma unroll
    for (int kt = 0; kt < 4; ++kt)
#pragma unroll
      for (int r = 0; r < 4; ++r) { m0 = fmaxf(m0, S0[i][kt][r]); m1 = fmaxf(m1, S1[i][kt][r]); }
#pragma unroll
    for (int r = 0; r < 4; ++r) m9 = fmaxf(m9, S9[i][r]);
  }
  m0 = xmax(m0); m1 = xmax(m1); m9 = xmax(m9);
  float* xch = (float*)(smem + SCR_OFF);
  if (g == 0) xch[w * 16 + c] = m9;
  wait_bar<63>();
  m9 = fmaxf(fmaxf(xch[c], xch[16 + c]), fmaxf(xch[32 + c], xch[48 + c]));
  const float n0 = -m0 * SM_C, n1 = -m1 * SM_C, n9 = -m9 * SM_C;
  float l0 = 0.f, l1 = 0.f, l9 = 0.f;
  bf16x8 P0[8][2], P1[8][2]; bf16x4 P9[8];
#pragma unroll
  for (int i = 0; i < 8; ++i) {
#pragma unroll
    for (int u = 0; u < 2; ++u) {
      f32x4 a = S0[i][2 * u], bq = S0[i][2 * u + 1];
#pragma unroll
      for (int r = 0; r < 4; ++r) { a[r] = __builtin_amdgcn_exp2f(fmaf(a[r], SM_C, n0)); bq[r] = __builtin_amdgcn_exp2f(fmaf(bq[r], SM_C, n0)); }
      l0 += (a[0] + a[1]) + (a[2] + a[3]) + (bq[0] + bq[1]) + (bq[2] + bq[3]);
      u32x4 pk = {cvtpk(a[0], a[1]), cvtpk(a[2], a[3]), cvtpk(bq[0], bq[1]), cvtpk(bq[2], bq[3])};
      P0[i][u] = *(bf16x8*)&pk;
      a = S1[i][2 * u]; bq = S1[i][2 * u + 1];
#pragma unroll
      for (int r = 0; r < 4; ++r) { a[r] = __builtin_amdgcn_exp2f(fmaf(a[r], SM_C, n1)); bq[r] = __builtin_amdgcn_exp2f(fmaf(bq[r], SM_C, n1)); }
      l1 += (a[0] + a[1]) + (a[2] + a[3]) + (bq[0] + bq[1]) + (bq[2] + bq[3]);
      u32x4 pk1 = {cvtpk(a[0], a[1]), cvtpk(a[2], a[3]), cvtpk(bq[0], bq[1]), cvtpk(bq[2], bq[3])};
      P1[i][u] = *(bf16x8*)&pk1;
    }
    f32x4 a = S9[i];
#pragma unroll
    for (int r = 0; r < 4; ++r) a[r] = __builtin_amdgcn_exp2f(fmaf(a[r], SM_C, n9));
    l9 += (a[0] + a[1]) + (a[2] + a[3]);
    u32x2 pk = {cvtpk(a[0], a[1]), cvtpk(a[2], a[3])};
    P9[i] = *(bf16x4*)&pk;
  }
  l0 = xsum(l0); l1 = xsum(l1); l9 = xsum(l9);
  if (g == 0) xch[64 + w * 16 + c] = l9;

  f32x4 O0[16], O1[16], O9[16];
#pragma unroll
  for (int t = 0; t < 16; ++t) { O0[t] = (f32x4){0, 0, 0, 0}; O1[t] = (f32x4){0, 0, 0, 0}; O9[t] = (f32x4){0, 0, 0, 0}; }
  int lane2 = threadIdx.x & 63; asm volatile("" : "+v"(lane2));
  const int g2 = lane2 >> 4, qq2 = (lane2 & 15) >> 2, pp2 = lane2 & 3;
  const int vsw = 4 * (g2 & 1) + qq2;
#pragma unroll
  for (int j = 0; j < 8; ++j) {
    if (j <= 4) wait_bar<16>(); else if (j == 5) wait_bar<16>(); else if (j == 6) wait_bar<8>(); else wait_bar<0>();
    if (j + 3 < 8) issue_kv<1>(X, j + 3, (j + 3) & 3, 0, 8);
    const unsigned sb = (j & 3) * SLOT;
#pragma unroll
    for (int u = 0; u < 2; ++u) {
      const int ra = 16 * ((2 * u + w) & 3) + 4 * g2 + qq2;
      const int rb = 16 * ((2 * u + 1 + w) & 3) + 4 * g2 + qq2;
#pragma unroll
      for (int t = 0; t < 16; ++t) {
        const bf16x4 lo = tr_rd(smem, sb + ra * 512 + ((t ^ vsw) * 32) + pp2 * 8);
        const bf16x4 hi = tr_rd(smem, sb + rb * 512 + ((t ^ vsw) * 32) + pp2 * 8);
        const bf16x8 a = cat8(lo, hi);
        O0[t] = MFMA32(a, P0[j][u], O0[t]);
        O1[t] = MFMA32(a, P1[j][u], O1[t]);
        if (u == 0) O9[t] = MFMA16(lo, P9[j], O9[t]);
      }
    }
  }

  int lane3 = threadIdx.x & 63; asm volatile("" : "+v"(lane3));
  const int c3 = lane3 & 15, g3 = lane3 >> 4;
  u16* ob = o_ws + (size_t)blk * (NB * SQ) * HD + (size_t)b * SQ * HD + h * DH;
  {
    const float r0 = 1.0f / l0, r1 = 1.0f / l1;
    u16* p0 = ob + (size_t)(32 * w + c3) * HD + 4 * g3;
    u16* p1 = p0 + (size_t)16 * HD;
#pragma unroll
    for (int t = 0; t < 16; ++t) {
      u32x2 v0 = {cvtpk(O0[t][0] * r0, O0[t][1] * r0), cvtpk(O0[t][2] * r0, O0[t][3] * r0)};
      u32x2 v1 = {cvtpk(O1[t][0] * r1, O1[t][1] * r1), cvtpk(O1[t][2] * r1, O1[t][3] * r1)};
      *(u32x2*)(p0 + 16 * t) = v0;
      *(u32x2*)(p1 + 16 * t) = v1;
    }
  }
  wait_bar<63>();
  {
    f32x4* red = (f32x4*)smem;
#pragma unroll
    for (int t = 0; t < 16; ++t) red[(w * 16 + t) * 64 + lane3] = O9[t];
    wait_bar<63>();
    const float lt = (xch[64 + c3] + xch[80 + c3]) + (xch[96 + c3] + xch[112 + c3]);
    const float r9 = 1.0f / lt;
    u16* p9 = ob + (size_t)(128 + c3) * HD + 4 * g3;
#pragma unroll
    for (int tt = 0; tt < 4; ++tt) {
      const int t = 4 * w + tt;
      f32x4 a = red[(0 * 16 + t) * 64 + lane3];
      f32x4 b1 = red[(1 * 16 + t) * 64 + lane3], b2 = red[(2 * 16 + t) * 64 + lane3], b3 = red[(3 * 16 + t) * 64 + lane3];
      a = (a + b1) + (b2 + b3);
      u32x2 v = {cvtpk(a[0] * r9, a[1] * r9), cvtpk(a[2] * r9, a[3] * r9)};
      *(u32x2*)(p9 + 16 * t) = v;
    }
  }
}

__global__ __launch_bounds__(256)
void out_proj(const u16* __restrict__ o_ws, const u16* __restrict__ woA, const u16* __restrict__ woB, u16* __restrict__ out) {
  __shared__ __attribute__((aligned(16))) u16 As[64][72];
  __shared__ __attribute__((aligned(16))) u16 Bs[64][72];
  const int bid = blockIdx.x;
  const int nt = bid % 7, mt = (bid / 7) % 18, blk = bid / 126;
  const u16* A = o_ws + (size_t)blk * (NB * SQ) * HD + (size_t)mt * 64 * HD;
  const u16* Bm = (blk ? woB : woA) + nt * 64;
  const int tid = threadIdx.x, lane = tid & 63, w = tid >> 6, c = lane & 15, g = lane >> 4;
  const int wm = (w >> 1) * 32, wn = (w & 1) * 32;
  f32x4 acc[2][2];
#pragma unroll
  for (int i = 0; i < 2; ++i)
#pragma unroll
    for (int j = 0; j < 2; ++j) acc[i][j] = (f32x4){0, 0, 0, 0};
  for (int k0 = 0; k0 < HD; k0 += 64) {
#pragma unroll
    for (int it = 0; it < 2; ++it) {
      const int idx = tid + it * 256, r = idx >> 3, ch = idx & 7;
      *(bf16x8*)&As[r][ch * 8] = *(const bf16x8*)(A + (size_t)r * HD + k0 + ch * 8);
      *(bf16x8*)&Bs[r][ch * 8] = *(const bf16x8*)(Bm + (size_t)(k0 + r) * DM + ch * 8);
    }
    __syncthreads();
#pragma unroll
    for (int ks = 0; ks < 2; ++ks) {
      bf16x8 af[2], bfr[2];
#pragma unroll
      for (int i = 0; i < 2; ++i) af[i] = *(const bf16x8*)&As[wm + 16 * i + c][ks * 32 + 8 * g];
#pragma unroll
      for (int j = 0; j < 2; ++j) {
        bf16x8 t;
#pragma unroll
        for (int e = 0; e < 8; ++e) t[e] = (short)Bs[ks * 32 + 8 * g + e][wn + 16 * j + c];
        bfr[j] = t;
      }
#pragma unroll
      for (int i = 0; i < 2; ++i)
#pragma unroll
        for (int j = 0; j < 2; ++j) acc[i][j] = MFMA32(af[i], bfr[j], acc[i][j]);
    }
    __syncthreads();
  }
#pragma unroll
  for (int i = 0; i < 2; ++i)
#pragma unroll
    for (int j = 0; j < 2; ++j)
#pragma unroll
      for (int r = 0; r < 4; ++r) {
        const int m = mt * 64 + wm + 16 * i + 4 * g + r, n = nt * 64 + wn + 16 * j + c;
        __hip_bfloat16 v = __float2bfloat16(acc[i][j][r]);
        out[(size_t)m * (2 * DM) + blk * DM + n] = *(u16*)&v;
      }
}

extern "C" void kernel_launch(void* const* d_in, const int* in_sizes, int n_in,
                              void* d_out, int out_size, void* d_ws, size_t ws_size,
                              hipStream_t stream) {
  const u16* xA = (const u16*)d_in[0]; const u16* kA = (const u16*)d_in[1]; const u16* vA = (const u16*)d_in[2];
  const u16* wqA = (const u16*)d_in[3]; const u16* woA = (const u16*)d_in[4];
  const u16* xB = (const u16*)d_in[5]; const u16* kB = (const u16*)d_in[6]; const u16* vB = (const u16*)d_in[7];
  const u16* wqB = (const u16*)d_in[8]; const u16* woB = (const u16*)d_in[9];
  u16* o_ws = (u16*)d_ws;
  attn_fused<<<256, 256, 0, stream>>>(xA, kA, vA, wqA, xB, kB, vB, wqB, o_ws);
  out_proj<<<252, 256, 0, stream>>>(o_ws, woA, woB, (u16*)d_out);
}
```

```cpp
#include <hip/hip_runtime.h>
#include <hip/hip_bf16.h>
#include <stdint.h>

typedef __attribute__((ext_vector_type(8))) short bf16x8;
typedef __attribute__((ext_vector_type(4))) short bf16x4;
typedef __attribute__((ext_vector_type(4))) float f32x4;
typedef __attribute__((ext_vector_type(2))) unsigned u32x2;
typedef __attribute__((ext_vector_type(4))) unsigned u32x4;
typedef unsigned short u16;

namespace {
constexpr int NB = 8, SQ = 144, DM = 448, NH = 16, SK = 512, DH = 256, HD = NH * DH;
constexpr int LDS_BYTES = 163840;
constexpr int SLOT = 32768;
constexpr int SLAB_OFF = 98304;
constexpr int SCR_OFF = 131072;
constexpr float SM_C = 0.0625f * 1.4426950408889634f;

#define MFMA32(a, b, c) __builtin_amdgcn_mfma_f32_16x16x32_bf16((a), (b), (c), 0, 0, 0)
#define MFMA16(a, b, c) __builtin_amdgcn_mfma_f32_16x16x16bf16_1k((a), (b), (c), 0, 0, 0)

__device__ __forceinline__ void dma16(const void* gbase  , unsigned voff, unsigned lds_dst) {
  unsigned keep;
  asm volatile("s_mov_b32 %0, m0\n\ts_mov_b32 m0, %3\n\ts_nop 0\n\tglobal_load_lds_dwordx4 %1, %2\n\ts_mov_b32 m0, %0"
               : "=&s"(keep) : "v"(voff), "s"(gbase), "s"(lds_dst) : "memory");
}
__device__ __forceinline__ void dma16nt(const void* gbase  , unsigned voff, unsigned lds_dst) {
  unsigned keep;
  asm volatile("s_mov_b32 %0, m0\n\ts_mov_b32 m0, %3\n\ts_nop 0\n\tglobal_load_lds_dwordx4 %1, %2 nt\n\ts_mov_b32 m0, %0"
               : "=&s"(keep) : "v"(voff), "s"(gbase), "s"(lds_dst) : "memory");
}
template <int N> __device__ __forceinline__ void wait_bar() {
  asm volatile("s_waitcnt vmcnt(%0) lgkmcnt(0)" :: "n"(N) : "memory");
  __builtin_amdgcn_s_barrier();
  asm volatile("" ::: "memory");
}
__device__ __forceinline__ unsigned cvtpk(float lo, float hi) {
  unsigned r; asm("v_cvt_pk_bf16_f32 %0, %1, %2" : "=v"(r) : "v"(lo), "v"(hi)); return r;
}
__device__ __forceinline__ bf16x4 tr_rd(const char* smem, unsigned off) {
  return __builtin_amdgcn_ds_read_tr16_b64_v4i16((__attribute__((address_space(3))) bf16x4*)(smem + off));
}
__device__ __forceinline__ bf16x8 cat8(bf16x4 a, bf16x4 b) {
  return (bf16x8){a[0], a[1], a[2], a[3], b[0], b[1], b[2], b[3]};
}
__device__ __forceinline__ float xmax(float v) {
  v = fmaxf(v, __shfl_xor(v, 16)); v = fmaxf(v, __shfl_xor(v, 32)); return v;
}
__device__ __forceinline__ float xsum(float v) {
  v += __shfl_xor(v, 16); v += __shfl_xor(v, 32); return v;
}

#define KPRE 1
#define ROT 0
constexpr int kq_cnt(int s) { return (KPRE && s >= 0 && s <= 11) ? 2 : 0; }
constexpr int slab_cnt(int s) { return (s + 3 <= 13) ? 7 : 0; }
constexpr int younger_than_slab(int s) {
  int n = 0;
  if (s < 3) { n += (2 - s) * 7; for (int t = 0; t < s; ++t) n += slab_cnt(t) + kq_cnt(t); }
  else { n += kq_cnt(s - 3); for (int t = s - 2; t < s; ++t) n += slab_cnt(t) + kq_cnt(t); }
  return n;
}
}

#define STAMP(i) do {} while (0)
struct Ctx {
  char* smem;
  unsigned smem_base;
  int w, lane, c, g, rot;
  const char* kv[2];
};

template <int ISV>
__device__ __forceinline__ void issue_kv(const Ctx& X, int chunk, int slot, int n0, int n1) {
  const char* base = X.kv[ISV] + (size_t)((chunk + X.rot) & 7) * SLOT;
  int ln = X.lane; asm volatile("" : "+v"(ln));
#pragma unroll
  for (int n = n0; n < n1; ++n) {
    const int row = 16 * X.w + 2 * n + (ln >> 5);
    const int p = ln & 31;
    const int sc = p ^ ((row & 7) << 1);
    dma16nt(base, row * 512 + sc * 16, X.smem_base + slot * SLOT + X.w * 8192 + n * 1024);
  }
}

__global__ __launch_bounds__(256, 1)
void attn_fused(const u16* __restrict__ xA, const u16* __restrict__ kA, const u16* __restrict__ vA, const u16* __restrict__ wqA,
                const u16* __restrict__ xB, const u16* __restrict__ kB, const u16* __restrict__ vB, const u16* __restrict__ wqB,
                u16* __restrict__ o_ws) {
  __shared__ __attribute__((aligned(16))) char smem[LDS_BYTES];
  const int bid = blockIdx.x;
  const int h = bid & 15, b = (bid >> 4) & 7, blk = bid >> 7;
  const u16* x = (blk ? xB : xA) + (size_t)b * SQ * DM;
  const u16* wq = (blk ? wqB : wqA) + h * DH;
  Ctx X;
  X.smem = smem;
  X.smem_base = (unsigned)(uintptr_t)smem;
  X.w = __builtin_amdgcn_readfirstlane(threadIdx.x >> 6);
  X.rot = ROT ? ((bid >> 3) & 7) : 0;
  X.lane = threadIdx.x & 63; X.c = X.lane & 15; X.g = X.lane >> 4;
  X.kv[0] = (const char*)((blk ? kB : kA) + (size_t)(b * NH + h) * SK * DH);
  X.kv[1] = (const char*)((blk ? vB : vA) + (size_t)(b * NH + h) * SK * DH);
  const int w = X.w, lane = X.lane, c = X.c, g = X.g;
  STAMP(0);

  bf16x8 xf[4][3];
  const char* xb = (const char*)x;
  auto issue_x = [&](int s) {
    int ln = lane; asm volatile("" : "+v"(ln));
    const unsigned o0 = (unsigned)(32 * w + (ln & 15)) * (DM * 2) + (ln >> 4) * 16 + s * 64;
    const unsigned o9 = (unsigned)(128 + (ln & 15)) * (DM * 2) + (ln >> 4) * 16 + s * 64;
    asm volatile("global_load_dwordx4 %0, %1, %2" : "=v"(xf[s & 3][0]) : "v"(o0), "s"(xb) : "memory");
    asm volatile("global_load_dwordx4 %0, %1, %2" : "=v"(xf[s & 3][1]) : "v"(o0 + 16 * DM * 2), "s"(xb) : "memory");
    asm volatile("global_load_dwordx4 %0, %1, %2" : "=v"(xf[s & 3][2]) : "v"(o9), "s"(xb) : "memory");
  };
  auto issue_slab = [&](int s) {
    int ln = lane; asm volatile("" : "+v"(ln));
#pragma unroll
    for (int n = 0; n < 4; ++n) {
      const int row = 8 * w + 2 * n + (ln >> 5);
      const int p = ln & 31;
      const int f = (row & 3) | (((row >> 3) & 1) << 2);
      const int sc = (((p >> 1) ^ f) << 1) | (p & 1);
      dma16((const char*)(wq + (size_t)(32 * s) * HD), row * (HD * 2) + sc * 16,
            X.smem_base + SLAB_OFF + (s & 3) * 16384 + w * 4096 + n * 1024);
    }
  };
  issue_x(0); issue_slab(0); issue_x(1); issue_slab(1); issue_x(2); issue_slab(2);

  f32x4 qa0[16], qa1[16], qa9[4];
#pragma unroll
  for (int t = 0; t < 16; ++t) { qa0[t] = (f32x4){0, 0, 0, 0}; qa1[t] = (f32x4){0, 0, 0, 0}; }
#pragma unroll
  for (int t = 0; t < 4; ++t) qa9[t] = (f32x4){0, 0, 0, 0};

  const int qq = c >> 2, pp = c & 3;
#pragma unroll
  for (int s = 0; s < 14; ++s) {
    switch (s) {
#define WB(S) case S: wait_bar<younger_than_slab(S)>(); break;
      WB(0) WB(1) WB(2) WB(3) WB(4) WB(5) WB(6) WB(7) WB(8) WB(9) WB(10) WB(11) WB(12) WB(13)
#undef WB
    }
    if (s + 3 <= 13) { issue_x(s + 3); issue_slab(s + 3); }
    if (KPRE && s <= 11) issue_kv<0>(X, s >> 2, s >> 2, 2 * (s & 3), 2 * (s & 3) + 2);
    const unsigned sb = SLAB_OFF + (s & 3) * 16384;
    const int row = 8 * g + qq;
    const int f = (row & 3) | (((row >> 3) & 1) << 2);
    const int row1 = row + 4;
    const int f1 = (row1 & 3) | (((row1 >> 3) & 1) << 2);
    auto rdA = [&](int t) -> bf16x8 {
      const int ta = (t + 4 * w) & 15;
      const bf16x4 a0 = tr_rd(smem, sb + row * 512 + ((ta ^ f) * 32) + pp * 8);
      const bf16x4 a1 = tr_rd(smem, sb + row1 * 512 + ((ta ^ f1) * 32) + pp * 8);
      return cat8(a0, a1);
    };
    bf16x8 fr[4];
#pragma unroll
    for (int t = 0; t < 4; ++t) fr[t] = rdA(t);
    __builtin_amdgcn_sched_barrier(0);
#pragma unroll
    for (int t = 0; t < 16; ++t) {
      const bf16x8 a = fr[t & 3];
      qa0[t] = MFMA32(a, xf[s & 3][0], qa0[t]);
      qa1[t] = MFMA32(a, xf[s & 3][1], qa1[t]);
      if (t < 4) qa9[t] = MFMA32(a, xf[s & 3][2], qa9[t]);
      if (t + 4 < 16) fr[t & 3] = rdA(t + 4);
      __builtin_amdgcn_sched_barrier(0);
    }
  }

  STAMP(1);
  if (!KPRE) { issue_kv<0>(X, 0, 0, 0, 8); issue_kv<0>(X, 1, 1, 0, 8); issue_kv<0>(X, 2, 2, 0, 8); }
  bf16x8 q0[8], q1[8], q9[8];
  {
    char* pr = smem + SCR_OFF + w * 8192;
#pragma unroll
    for (int t = 0; t < 16; ++t) {
      const int ta = (t + 4 * w) & 15;
      const int ch = 2 * ta + (g >> 1);
      u32x2 v = {cvtpk(qa0[t][0], qa0[t][1]), cvtpk(qa0[t][2], qa0[t][3])};
      *(u32x2*)(pr + c * 512 + ((ch ^ c) * 16) + (g & 1) * 8) = v;
    }
#pragma unroll
    for (int ks = 0; ks < 8; ++ks) q0[ks] = *(const bf16x8*)(pr + c * 512 + (((4 * ks + g) ^ c) * 16));
    asm volatile("s_waitcnt lgkmcnt(0)" ::: "memory");
#pragma unroll
    for (int t = 0; t < 16; ++t) {
      const int ta = (t + 4 * w) & 15;
      const int ch = 2 * ta + (g >> 1);
      u32x2 v = {cvtpk(qa1[t][0], qa1[t][1]), cvtpk(qa1[t][2], qa1[t][3])};
      *(u32x2*)(pr + c * 512 + ((ch ^ c) * 16) + (g & 1) * 8) = v;
    }
#pragma unroll
    for (int ks = 0; ks < 8; ++ks) q1[ks] = *(const bf16x8*)(pr + c * 512 + (((4 * ks + g) ^ c) * 16));
    wait_bar<63>();
    char* sh = smem + SLAB_OFF;
#pragma unroll
    for (int t = 0; t < 4; ++t) {
      const int ta = t + 4 * w;
      const int ch = 2 * ta + (g >> 1);
      u32x2 v = {cvtpk(qa9[t][0], qa9[t][1]), cvtpk(qa9[t][2], qa9[t][3])};
      *(u32x2*)(sh + c * 512 + ((ch ^ c) * 16) + (g & 1) * 8) = v;
    }
    wait_bar<63>();
#pragma unroll
    for (int ks = 0; ks < 8; ++ks) q9[ks] = *(const bf16x8*)(sh + c * 512 + (((4 * ks + g) ^ c) * 16));
  }

  STAMP(2);
  f32x4 S0[8][4], S1[8][4], S9[8];
#pragma unroll
  for (int i = 0; i < 8; ++i) {
    wait_bar<16>();
    if (i + 3 < 8) issue_kv<0>(X, i + 3, (i + 3) & 3, 0, 8);
    else issue_kv<1>(X, i + 3 - 8, (i + 3) & 3, 0, 8);
    const unsigned sb = (i & 3) * SLOT;
    int lk = threadIdx.x & 63; asm volatile("" : "+v"(lk));
    const int ck = lk & 15, gk = lk >> 4;
    auto rdK = [&](int n) -> bf16x8 {
      const int kt = n >> 3, ks = n & 7;
      const int kta = (kt + w) & 3;
      return *(const bf16x8*)(smem + sb + (16 * kta + ck) * 512 + (((4 * ks + gk) ^ ((ck & 7) << 1)) * 16));
    };
    bf16x8 fr[4];
#pragma unroll
    for (int n = 0; n < 4; ++n) fr[n] = rdK(n);
    __builtin_amdgcn_sched_barrier(0);
#pragma unroll
    for (int kt = 0; kt < 4; ++kt) {
      f32x4 s0 = {0, 0, 0, 0}, s1 = {0, 0, 0, 0}, s9 = {0, 0, 0, 0};
#pragma unroll
      for (int ks = 0; ks < 8; ++ks) {
        const int n = kt * 8 + ks;
        const bf16x8 a = fr[n & 3];
        s0 = MFMA32(a, q0[ks], s0);
        s1 = MFMA32(a, q1[ks], s1);
        if (kt == 0) s9 = MFMA32(a, q9[ks], s9);
        if (n + 4 < 32) fr[n & 3] = rdK(n + 4);
        __builtin_amdgcn_sched_barrier(0);
      }
      S0[i][kt] = s0; S1[i][kt] = s1;
      if (kt == 0) S9[i] = s9;
    }
  }

  STAMP(3);
  float m0 = S0[0][0][0], m1 = S1[0][0][0], m9 = S9[0][0];
#pragma unroll
  for (int i = 0; i < 8; ++i) {
#pragma unroll
    for (int kt = 0; kt < 4; ++kt)
#pragma unroll
      for (int r = 0; r < 4; ++r) { m0 = fmaxf(m0, S0[i][kt][r]); m1 = fmaxf(m1, S1[i][kt][r]); }
#pragma unroll
    for (int r = 0; r < 4; ++r) m9 = fmaxf(m9, S9[i][r]);
  }
  m0 = xmax(m0); m1 = xmax(m1); m9 = xmax(m9);
  float* xch = (float*)(smem + SCR_OFF);
  if (g == 0) xch[w * 16 + c] = m9;
  wait_bar<63>();
  m9 = fmaxf(fmaxf(xch[c], xch[16 + c]), fmaxf(xch[32 + c], xch[48 + c]));
  const float n0 = -m0 * SM_C, n1 = -m1 * SM_C, n9 = -m9 * SM_C;
  float l0 = 0.f, l1 = 0.f, l9 = 0.f;
  bf16x8 P0[8][2], P1[8][2]; bf16x4 P9[8];
  auto conv = [&](int j) {
#pragma unroll
    for (int u = 0; u < 2; ++u) {
      f32x4 a = S0[j][2 * u], bq = S0[j][2 * u + 1];
#pragma unroll
      for (int r = 0; r < 4; ++r) { a[r] = __builtin_amdgcn_exp2f(fmaf(a[r], SM_C, n0)); bq[r] = __builtin_amdgcn_exp2f(fmaf(bq[r], SM_C, n0)); }
      l0 += (a[0] + a[1]) + (a[2] + a[3]) + (bq[0] + bq[1]) + (bq[2] + bq[3]);
      u32x4 pk = {cvtpk(a[0], a[1]), cvtpk(a[2], a[3]), cvtpk(bq[0], bq[1]), cvtpk(bq[2], bq[3])};
      P0[j][u] = *(bf16x8*)&pk;
      a = S1[j][2 * u]; bq = S1[j][2 * u + 1];
#pragma unroll
      for (int r = 0; r < 4; ++r) { a[r] = __builtin_amdgcn_exp2f(fmaf(a[r], SM_C, n1)); bq[r] = __builtin_amdgcn_exp2f(fmaf(bq[r], SM_C, n1)); }
      l1 += (a[0] + a[1]) + (a[2] + a[3]) + (bq[0] + bq[1]) + (bq[2] + bq[3]);
      u32x4 pk1 = {cvtpk(a[0], a[1]), cvtpk(a[2], a[3]), cvtpk(bq[0], bq[1]), cvtpk(bq[2], bq[3])};
      P1[j][u] = *(bf16x8*)&pk1;
    }
    f32x4 a = S9[j];
#pragma unroll
    for (int r = 0; r < 4; ++r) a[r] = __builtin_amdgcn_exp2f(fmaf(a[r], SM_C, n9));
    l9 += (a[0] + a[1]) + (a[2] + a[3]);
    u32x2 pk = {cvtpk(a[0], a[1]), cvtpk(a[2], a[3])};
    P9[j] = *(bf16x4*)&pk;
  };
  conv(0); conv(1); conv(2); conv(3); conv(4); conv(5); conv(6); conv(7);
  l0 = xsum(l0); l1 = xsum(l1); l9 = xsum(l9);
  if (g == 0) xch[64 + w * 16 + c] = l9;
  STAMP(4);
  f32x4 O0[16], O1[16], O9[16];
#pragma unroll
  for (int t = 0; t < 16; ++t) { O0[t] = (f32x4){0, 0, 0, 0}; O1[t] = (f32x4){0, 0, 0, 0}; O9[t] = (f32x4){0, 0, 0, 0}; }
  int lane2 = threadIdx.x & 63; asm volatile("" : "+v"(lane2));
  const int g2 = lane2 >> 4, qq2 = (lane2 & 15) >> 2, pp2 = lane2 & 3;
  const int vsw = 4 * (g2 & 1) + qq2;
#pragma unroll
  for (int j = 0; j < 8; ++j) {
    if (j <= 4) wait_bar<16>(); else if (j == 5) wait_bar<16>(); else if (j == 6) wait_bar<8>(); else wait_bar<0>();
    if (j + 3 < 8) issue_kv<1>(X, j + 3, (j + 3) & 3, 0, 8);
    const unsigned sb = (j & 3) * SLOT;
    bf16x4 flo[4], fhi[4];
    auto rdV = [&](int n) {
      const int u = n >> 4, t = n & 15;
      const int ra = 16 * ((2 * u + w) & 3) + 4 * g2 + qq2;
      const int rb = 16 * ((2 * u + 1 + w) & 3) + 4 * g2 + qq2;
      flo[n & 3] = tr_rd(smem, sb + ra * 512 + ((t ^ vsw) * 32) + pp2 * 8);
      fhi[n & 3] = tr_rd(smem, sb + rb * 512 + ((t ^ vsw) * 32) + pp2 * 8);
    };

#pragma unroll
    for (int n = 0; n < 4; ++n) rdV(n);
    __builtin_amdgcn_sched_barrier(0);
#pragma unroll
    for (int n = 0; n < 32; ++n) {
      const int u = n >> 4, t = n & 15;
      const bf16x4 lo = flo[n & 3];
      const bf16x8 a = cat8(lo, fhi[n & 3]);
      O0[t] = MFMA32(a, P0[j][u], O0[t]);
      O1[t] = MFMA32(a, P1[j][u], O1[t]);
      if (u == 0) O9[t] = MFMA16(lo, P9[j], O9[t]);
      if (n + 4 < 32) rdV(n + 4);
      __builtin_amdgcn_sched_barrier(0);
    }
  }

  STAMP(5);
  int lane3 = threadIdx.x & 63; asm volatile("" : "+v"(lane3));
  const int c3 = lane3 & 15, g3 = lane3 >> 4;
  u16* ob = o_ws + (size_t)blk * (NB * SQ) * HD + (size_t)b * SQ * HD + h * DH;
  {
    char* pr = smem + w * 8192;
    const float r0 = 1.0f / l0, r1 = 1.0f / l1;
#pragma unroll
    for (int tl = 0; tl < 2; ++tl) {
      const float rr = tl ? r1 : r0;
#pragma unroll
      for (int t = 0; t < 16; ++t) {
        const f32x4 ov = tl ? O1[t] : O0[t];
        u32x2 v = {cvtpk(ov[0] * rr, ov[1] * rr), cvtpk(ov[2] * rr, ov[3] * rr)};
        *(u32x2*)(pr + c3 * 512 + (((2 * t + (g3 >> 1)) ^ c3) * 16) + (g3 & 1) * 8) = v;
      }
      asm volatile("s_waitcnt lgkmcnt(0)" ::: "memory");
#pragma unroll
      for (int it = 0; it < 8; ++it) {
        const int row = 2 * it + (lane3 >> 5), ch = lane3 & 31;
        const u32x4 v = *(const u32x4*)(pr + row * 512 + ((ch ^ row) * 16));
        *(u32x4*)(ob + (size_t)(32 * w + 16 * tl + row) * HD + ch * 8) = v;
      }
      asm volatile("s_waitcnt lgkmcnt(0)" ::: "memory");
    }
  }
  wait_bar<63>();
  {
    f32x4* red = (f32x4*)(smem + 2 * SLOT);
#pragma unroll
    for (int t = 0; t < 16; ++t) red[(w * 16 + t) * 64 + lane3] = O9[t];
    wait_bar<63>();
    const float lt = (xch[64 + c3] + xch[80 + c3]) + (xch[96 + c3] + xch[112 + c3]);
    const float r9 = 1.0f / lt;
    u16* p9 = ob + (size_t)(128 + c3) * HD + 4 * g3;
#pragma unroll
    for (int tt = 0; tt < 4; ++tt) {
      const int t = 4 * w + tt;
      f32x4 a = red[(0 * 16 + t) * 64 + lane3];
      f32x4 b1 = red[(1 * 16 + t) * 64 + lane3], b2 = red[(2 * 16 + t) * 64 + lane3], b3 = red[(3 * 16 + t) * 64 + lane3];
      a = (a + b1) + (b2 + b3);
      u32x2 v = {cvtpk(a[0] * r9, a[1] * r9), cvtpk(a[2] * r9, a[3] * r9)};
      *(u32x2*)(p9 + 16 * t) = v;
    }
  }
}

__global__ __launch_bounds__(256, 1)
void out_proj(const u16* __restrict__ o_ws, const u16* __restrict__ woA, const u16* __restrict__ woB, u16* __restrict__ out) {
  __shared__ __attribute__((aligned(16))) char sm[131072];
  const int bid = blockIdx.x;
  const int nt = bid % 7, mt = (bid / 7) % 18, blk = bid / 126;
  const int w = __builtin_amdgcn_readfirstlane(threadIdx.x >> 6);
  const int lane = threadIdx.x & 63, c = lane & 15, g = lane >> 4, qq = c >> 2, pp = c & 3;
  const char* A = (const char*)(o_ws + (size_t)blk * (NB * SQ) * HD + (size_t)mt * 64 * HD + w * 1024);
  const char* Bm = (const char*)((blk ? woB : woA) + (size_t)w * 1024 * DM + nt * 64);
  const unsigned base = (unsigned)(uintptr_t)sm + w * 32768;
  char* my = sm + w * 32768;
  auto issue = [&](int st) {
    int ln = lane; asm volatile("" : "+v"(ln));
    const int p = ln & 7;
#pragma unroll
    for (int n = 0; n < 8; ++n) {
      const int row = 8 * n + (ln >> 3);
      dma16(A + st * 128, row * (HD * 2) + ((p ^ (row & 7)) * 16), base + (st & 1) * 16384 + n * 1024);
      const int f = ((row >> 1) & 1) | (((row >> 3) & 1) << 1);
      dma16(Bm + (size_t)st * 64 * DM * 2, row * (DM * 2) + (((((p >> 1) ^ f) << 1) | (p & 1)) * 16), base + (st & 1) * 16384 + 8192 + n * 1024);
    }
  };
  f32x4 acc[4][4];
#pragma unroll
  for (int i = 0; i < 4; ++i)
#pragma unroll
    for (int j = 0; j < 4; ++j) acc[i][j] = (f32x4){0, 0, 0, 0};
  issue(0); issue(1);
  for (int st = 0; st < 16; ++st) {
    if (st < 15) asm volatile("s_waitcnt vmcnt(16)" ::: "memory"); else asm volatile("s_waitcnt vmcnt(0)" ::: "memory");
    const char* as = my + (st & 1) * 16384;
    const char* bs = as + 8192;
#pragma unroll
    for (int ks = 0; ks < 2; ++ks) {
      bf16x8 af[4], bf[4];
#pragma unroll
      for (int i = 0; i < 4; ++i) { const int row = 16 * i + c; af[i] = *(const bf16x8*)(as + row * 128 + (((4 * ks + g) ^ (row & 7)) * 16)); }
#pragma unroll
      for (int j = 0; j < 4; ++j) {
        const int r0 = 32 * ks + 8 * g + qq, r1 = r0 + 4;
        const int f0 = ((r0 >> 1) & 1) | (((r0 >> 3) & 1) << 1), f1 = ((r1 >> 1) & 1) | (((r1 >> 3) & 1) << 1);
        bf[j] = cat8(tr_rd(bs, r0 * 128 + ((j ^ f0) * 32) + pp * 8), tr_rd(bs, r1 * 128 + ((j ^ f1) * 32) + pp * 8));
      }
#pragma unroll
      for (int i = 0; i < 4; ++i)
#pragma unroll
        for (int j = 0; j < 4; ++j) acc[i][j] = MFMA32(af[i], bf[j], acc[i][j]);
    }
    asm volatile("s_waitcnt lgkmcnt(0)" ::: "memory");
    if (st + 2 < 16) issue(st + 2);
  }
  __syncthreads();
  f32x4* red = (f32x4*)sm;
#pragma unroll
  for (int i = 0; i < 4; ++i)
#pragma unroll
    for (int j = 0; j < 4; ++j) red[(w * 16 + i * 4 + j) * 64 + lane] = acc[i][j];
  __syncthreads();
#pragma unroll
  for (int j = 0; j < 4; ++j) {
    const int t = w * 4 + j;
    f32x4 a = (red[(0 * 16 + t) * 64 + lane] + red[(1 * 16 + t) * 64 + lane]) + (red[(2 * 16 + t) * 64 + lane] + red[(3 * 16 + t) * 64 + lane]);
#pragma unroll
    for (int r = 0; r < 4; ++r) {
      const int m = mt * 64 + 16 * w + 4 * g + r, n = nt * 64 + 16 * j + c;
      __hip_bfloat16 v = __float2bfloat16(a[r]);
      out[(size_t)m * (2 * DM) + blk * DM + n] = *(u16*)&v;
    }
  }
}

extern "C" void kernel_launch(void* const* d_in, const int* in_sizes, int n_in,
                              void* d_out, int out_size, void* d_ws, size_t ws_size,
                              hipStream_t stream) {
  const u16* xA = (const u16*)d_in[0]; const u16* kA = (const u16*)d_in[1]; const u16* vA = (const u16*)d_in[2];
  const u16* wqA = (const u16*)d_in[3]; const u16* woA = (const u16*)d_in[4];
  const u16* xB = (const u16*)d_in[5]; const u16* kB = (const u16*)d_in[6]; const u16* vB = (const u16*)d_in[7];
  const u16* wqB = (const u16*)d_in[8]; const u16* woB = (const u16*)d_in[9];
  u16* o_ws = (u16*)d_ws;
  attn_fused<<<256, 256, 0, stream>>>(xA, kA, vA, wqA, xB, kB, vB, wqB, o_ws);
  out_proj<<<252, 256, 0, stream>>>(o_ws, woA, woB, (u16*)d_out);
}
```
